```python
import torch

def _softmax(x, axis=-1):
    return torch.softmax(x, dim=axis)
def _gelu(x):
    return torch.nn.functional.gelu(x, approximate="tanh")
def _silu(x):
    return torch.nn.functional.silu(x)
def _sigmoid(x):
    return torch.sigmoid(x)
def _layernorm(x, axis=-1, eps=1e-5):
    m = x.mean(dim=axis, keepdim=True)
    v = x.var(dim=axis, keepdim=True, unbiased=False)
    return (x - m) / torch.sqrt(v + eps)
def _scatter_add(tgt, idx, src, axis=0):
    return tgt.clone().index_add_(axis, idx, src)
def _segment_sum(src, seg, n):
    out = torch.zeros((n,) + src.shape[1:], dtype=src.dtype)
    return out.index_add_(0, seg, src)
def _pad(x, pw):
    flat = tuple(v for pair in reversed(pw) for v in pair)
    return torch.nn.functional.pad(x, flat)

kernel_name = 'concat_heads__linear_kernel_hedgehog__spectral_fnet__prompt'

def setup_inputs():

    _w = (lambda a: (lambda t: t.to(torch.bfloat16) if t.is_floating_point() else t)(torch.as_tensor(a)))
    def _A_setup():
        B, S, D, H = 1, 2048, 512, 32
        Dh = D // H
        return {
            "x": _w((lambda *s: torch.randn(*s, dtype=torch.bfloat16) * 0.2)(B, S, D)),
            "wqkv": _w((lambda *s: torch.randn(*s, dtype=torch.bfloat16) * 1.6)(D, 3 * D)),
            "w_phi": _w((lambda *s: torch.randn(*s, dtype=torch.bfloat16) * 1.6)(H, Dh, Dh) * (Dh ** -0.5)),
            "wo": _w((lambda *s: torch.randn(*s, dtype=torch.bfloat16) * 0.8)(D, D)),
        }
    _A_in = _A_setup()

    def _B_setup():
        B, S, D = 1, 2048, 512
        ns = torch.arange(S)[:, None] * torch.arange(S)[None, :]
        nd = torch.arange(D)[:, None] * torch.arange(D)[None, :]
        Ws_re = torch.cos(-2.0 * torch.pi * ns / S).to(float)
        Ws_im = torch.sin(-2.0 * torch.pi * ns / S).to(float)
        Wd_re = torch.cos(-2.0 * torch.pi * nd / D).to(float)
        Wd_im = torch.sin(-2.0 * torch.pi * nd / D).to(float)
        return {
            "x": _w((lambda *s: torch.randn(*s, dtype=torch.bfloat16) * 0.05)(B, S, D)),
            "dft_seq_re": _w(Ws_re),
            "dft_seq_im": _w(Ws_im),
            "dft_hid_re": _w(Wd_re),
            "dft_hid_im": _w(Wd_im),
        }
    _B_in = _B_setup()

    return {
        "A_x": _A_in["x"],
        "A_wqkv": _A_in["wqkv"],
        "A_w_phi": _A_in["w_phi"],
        "A_wo": _A_in["wo"],
        "B_x": _B_in["x"],
        "B_dft_seq_re": _B_in["dft_seq_re"],
        "B_dft_seq_im": _B_in["dft_seq_im"],
        "B_dft_hid_re": _B_in["dft_hid_re"],
        "B_dft_hid_im": _B_in["dft_hid_im"],
    }

def reference(A_x, A_wqkv, A_w_phi, A_wo, B_x, B_dft_seq_re, B_dft_seq_im, B_dft_hid_re, B_dft_hid_im):
    def _A_ref(x, wqkv, w_phi, wo):
        B, S, D = x.shape
        H = 32; Dh = D // H
        qkv = (lambda x, *d: x.permute(*d))((x @ wqkv).reshape(B, S, 3, H, Dh), 2, 0, 3, 1, 4)
        q, k, v = qkv[0], qkv[1], qkv[2]
        pq = torch.einsum("bhsd,hdf->bhsf", q, w_phi)
        pk = torch.einsum("bhsd,hdf->bhsf", k, w_phi)
        phi_q = torch.concatenate([_softmax(pq, axis=-1), _softmax(-pq, axis=-1)], axis=-1)
        phi_k = torch.concatenate([_softmax(pk, axis=-1), _softmax(-pk, axis=-1)], axis=-1)
        kv = torch.einsum("bhsf,bhsd->bhfd", phi_k, v)
        z  = torch.einsum("bhsf->bhf", phi_k)
        num = torch.einsum("bhqf,bhfd->bhqd", phi_q, kv)
        den = torch.einsum("bhqf,bhf->bhq", phi_q, z)[..., None] + 1e-6
        o = num / den
        return (lambda x, *d: x.permute(*d))(o, 0, 2, 1, 3).reshape(B, S, D) @ wo

    def _B_ref(x, dft_seq_re, dft_seq_im, dft_hid_re, dft_hid_im):
        a = torch.einsum("bsd,ed->bse", x, dft_hid_re)
        b = torch.einsum("bsd,ed->bse", x, dft_hid_im)
        y = torch.einsum("ts,bse->bte", dft_seq_re, a) - torch.einsum("ts,bse->bte", dft_seq_im, b)
        return y

    a = _A_ref(A_x, A_wqkv, A_w_phi, A_wo)
    b = _B_ref(B_x, B_dft_seq_re, B_dft_seq_im, B_dft_hid_re, B_dft_hid_im)
    return torch.concatenate([a, b], axis=-1)
```

```cpp
#include <hip/hip_runtime.h>
#include <hip/hip_bf16.h>
#include <stdint.h>

typedef unsigned short u16;
typedef __attribute__((ext_vector_type(8))) short s16x8;
typedef __attribute__((ext_vector_type(4))) short s16x4;
typedef __attribute__((ext_vector_type(8))) __bf16 bf16x8;
typedef __attribute__((ext_vector_type(2))) __bf16 bf16x2;
typedef __attribute__((ext_vector_type(16))) float f32x16;
typedef __attribute__((ext_vector_type(4))) float f32x4;
typedef __attribute__((ext_vector_type(2))) float f32x2;
typedef __attribute__((ext_vector_type(4))) unsigned u32x4;
typedef __attribute__((ext_vector_type(2))) unsigned u32x2;
#define LAS __attribute__((address_space(3)))
#define DI __device__ __forceinline__

#define WAIT_V(n) asm volatile("s_waitcnt vmcnt(" #n ")" ::: "memory")
#define WAIT_L0() asm volatile("s_waitcnt lgkmcnt(0)" ::: "memory")
#define BAR() __builtin_amdgcn_s_barrier()
#define SCHED() __builtin_amdgcn_sched_barrier(0)

DI f32x16 mfma32(s16x8 a, s16x8 b, f32x16 c) { return __builtin_amdgcn_mfma_f32_32x32x16_bf16(__builtin_bit_cast(bf16x8, a), __builtin_bit_cast(bf16x8, b), c, 0, 0, 0); }
DI f32x4 mfma16(s16x8 a, s16x8 b, f32x4 c) { return __builtin_amdgcn_mfma_f32_16x16x32_bf16(__builtin_bit_cast(bf16x8, a), __builtin_bit_cast(bf16x8, b), c, 0, 0, 0); }
DI f32x4 mfma16k16(s16x4 a, s16x4 b, f32x4 c) { return __builtin_amdgcn_mfma_f32_16x16x16bf16_1k(a, b, c, 0, 0, 0); }
DI unsigned pk2(float a, float b) { f32x2 p = {a, b}; bf16x2 q = __builtin_convertvector(p, bf16x2); return __builtin_bit_cast(unsigned, q); }
DI s16x4 pk4(float a, float b, float c, float d) { u32x2 r = {pk2(a, b), pk2(c, d)}; return __builtin_bit_cast(s16x4, r); }
DI s16x4 trd(const char* p) { return __builtin_amdgcn_ds_read_tr16_b64_v4i16((LAS s16x4*)p); }
DI s16x8 cat(s16x4 a, s16x4 b) { s16x8 r; r.lo = a; r.hi = b; return r; }
DI void glds16(const void* g, char* l) { __builtin_amdgcn_global_load_lds((const unsigned*)g, (unsigned*)l, 16, 0, 0); }
DI float xmax4(float x) {
  auto s = __builtin_amdgcn_permlane16_swap(__float_as_uint(x), __float_as_uint(x), false, false);
  x = fmaxf(__uint_as_float(s[0]), __uint_as_float(s[1]));
  auto t = __builtin_amdgcn_permlane32_swap(__float_as_uint(x), __float_as_uint(x), false, false);
  return fmaxf(__uint_as_float(t[0]), __uint_as_float(t[1]));
}
DI float xmin4(float x) {
  auto s = __builtin_amdgcn_permlane16_swap(__float_as_uint(x), __float_as_uint(x), false, false);
  x = fminf(__uint_as_float(s[0]), __uint_as_float(s[1]));
  auto t = __builtin_amdgcn_permlane32_swap(__float_as_uint(x), __float_as_uint(x), false, false);
  return fminf(__uint_as_float(t[0]), __uint_as_float(t[1]));
}
DI float xsum4(float x) {
  auto s = __builtin_amdgcn_permlane16_swap(__float_as_uint(x), __float_as_uint(x), false, false);
  x = __uint_as_float(s[0]) + __uint_as_float(s[1]);
  auto t = __builtin_amdgcn_permlane32_swap(__float_as_uint(x), __float_as_uint(x), false, false);
  return __uint_as_float(t[0]) + __uint_as_float(t[1]);
}

constexpr size_t WS_PQ = 0;
constexpr size_t WS_PHIQ = 4u << 20;
constexpr size_t WS_KVP = 8u << 20;
constexpr size_t WS_ZP = 9u << 20;
constexpr size_t WS_KVF = 10u << 20;
constexpr size_t WS_ZF = (10u << 20) + 65536;

struct Params {
  const u16 *Ax, *Wqkv, *Wphi, *Wo, *Bx, *Wsr, *Wsi, *Wdr, *Wdi;
  u16* out; char* ws;
};

DI void bbig(const Params& p, int tb, int ri, int cb, char* lds) {
  const int tid = threadIdx.x, lane = tid & 63, w = __builtin_amdgcn_readfirstlane(tid >> 6);
  const int g = w >> 2, wt = (w >> 1) & 1, wd = w & 1;
  const u16* Ws = (ri ? p.Wsi : p.Wsr) + (size_t)tb * 128 * 2048;
  const u16* Xc = p.Bx + cb * 128;
  const u16* srcW[2]; const u16* srcX[2];
#pragma unroll
  for (int i = 0; i < 2; ++i) {
    const int j = 2 * w + i;
    const int r = 8 * j + (lane >> 3);
    srcW[i] = Ws + r * 2048 + (((lane & 7) ^ ((r >> 1) & 7)) << 3);
    const int k = 8 * (j & 7) + (lane >> 3);
    srcX[i] = Xc + k * 512 + (j >> 3) * 64 + (((lane & 7) ^ (4 * ((k >> 1) & 1) + 2 * ((k >> 3) & 1))) << 3);
  }
  auto issue = [&](int st) {
    char* b = lds + (st & 3) * 32768 + w * 2048;
    glds16(srcW[0] + st * 64, b); glds16(srcW[1] + st * 64, b + 1024);
    glds16(srcX[0] + st * 64 * 512, b + 16384); glds16(srcX[1] + st * 64 * 512, b + 16384 + 1024);
  };
  const int h = lane >> 5, r32 = lane & 31;
  const int sw = (r32 >> 1) & 7;
  int aW[2];
#pragma unroll
  for (int ks = 0; ks < 2; ++ks) aW[ks] = (64 * wt + r32) * 128 + (((4 * g + 2 * ks + h) ^ sw) << 4);
  const int blk = (lane >> 4) & 1, q = (lane & 15) >> 2, pp = lane & 3;
  int aX[2];
#pragma unroll
  for (int di = 0; di < 2; ++di)
    aX[di] = 16384 + wd * 8192 + (32 * g + 8 * h + q) * 128 + (((4 * di + 2 * blk + (pp >> 1)) ^ (4 * ((q >> 1) & 1) + 2 * h)) << 4) + 8 * (pp & 1);
  s16x8 Wf[2][2][2], Xf[2][2][2];
  auto rdfr = [&](int set, int st) {
    const char* b = lds + (st & 3) * 32768;
#pragma unroll
    for (int ks = 0; ks < 2; ++ks) {
#pragma unroll
      for (int tj = 0; tj < 2; ++tj) Wf[set][tj][ks] = *(const s16x8*)(b + aW[ks] + tj * 4096);
#pragma unroll
      for (int di = 0; di < 2; ++di) Xf[set][di][ks] = cat(trd(b + aX[di] + ks * 2048), trd(b + aX[di] + ks * 2048 + 512));
    }
  };
  f32x16 acc[2][2] = {};
  auto mma = [&](int set) {
#pragma unroll
    for (int ks = 0; ks < 2; ++ks)
#pragma unroll
      for (int di = 0; di < 2; ++di)
#pragma unroll
        for (int tj = 0; tj < 2; ++tj) acc[di][tj] = mfma32(Xf[set][di][ks], Wf[set][tj][ks], acc[di][tj]);
  };
  issue(0); issue(1); issue(2); issue(3);
  WAIT_V(12); BAR();
  rdfr(0, 0); WAIT_L0();
#pragma unroll 1
  for (int i = 0; i < 28; i += 4) {
#pragma unroll
    for (int u = 0; u < 4; ++u) {
      WAIT_V(8); BAR(); SCHED();
      issue(i + u + 4);
      rdfr((u + 1) & 1, u + 1);
      mma(u & 1);
      WAIT_L0(); SCHED();
    }
  }
  WAIT_V(8); BAR(); SCHED(); rdfr(1, 29); mma(0); WAIT_L0(); SCHED();
  WAIT_V(4); BAR(); SCHED(); rdfr(0, 30); mma(1); WAIT_L0(); SCHED();
  WAIT_V(0); BAR(); SCHED(); rdfr(1, 31); mma(0); WAIT_L0(); SCHED();
  mma(1);
  BAR();
  {
    float* ex = (float*)lds + (size_t)w * 2048;
#pragma unroll
    for (int tj = 0; tj < 2; ++tj)
#pragma unroll
      for (int r4 = 0; r4 < 4; ++r4) {
        f32x4 v = g ? f32x4{acc[0][tj][4 * r4], acc[0][tj][4 * r4 + 1], acc[0][tj][4 * r4 + 2], acc[0][tj][4 * r4 + 3]}
                    : f32x4{acc[1][tj][4 * r4], acc[1][tj][4 * r4 + 1], acc[1][tj][4 * r4 + 2], acc[1][tj][4 * r4 + 3]};
        *(f32x4*)(ex + (tj * 4 + r4) * 256 + lane * 4) = v;
      }
  }
  __syncthreads();
  {
    const float* ex = (const float*)lds + (size_t)(w ^ 4) * 2048;
    char* stg = lds + 65536;
#pragma unroll
    for (int tj = 0; tj < 2; ++tj)
#pragma unroll
      for (int r4 = 0; r4 < 4; ++r4) {
        f32x4 v = *(const f32x4*)(ex + (tj * 4 + r4) * 256 + lane * 4);
#pragma unroll
        for (int e = 0; e < 4; ++e) v[e] += g ? acc[1][tj][4 * r4 + e] : acc[0][tj][4 * r4 + e];
        const int t = 64 * wt + 32 * tj + r32;
        const int d = 64 * wd + 32 * g + 8 * r4 + 4 * h;
        u32x2 o = {pk2(v[0], v[1]), pk2(v[2], v[3])};
        *(u32x2*)(stg + t * 256 + ((((d >> 3) ^ (t & 15))) << 4) + (d & 4) * 2) = o;
      }
  }
  __syncthreads();
  {
    const char* stg = lds + 65536;
    u16* dst = (u16*)(p.ws + WS_PQ) + (size_t)tb * 128 * 1024 + ri * 512 + cb * 128;
#pragma unroll
    for (int it = 0; it < 4; ++it) {
      const int t = it * 32 + (tid >> 4), c = tid & 15;
      u32x4 v = *(const u32x4*)(stg + t * 256 + ((c ^ (t & 15)) << 4));
      *(u32x4*)(dst + (size_t)t * 1024 + c * 8) = v;
    }
  }
}

DI void bsmall(const Params& p, int tb, int eb, char* lds) {
  const int tid = threadIdx.x, lane = tid & 63, w = __builtin_amdgcn_readfirstlane(tid >> 6);
  const int g = w >> 2, wt = (w >> 1) & 1, we = w & 1;
  const u16* src[4];
#pragma unroll
  for (int i = 0; i < 4; ++i) {
    const int j = (w & 3) * 4 + i;
    const int r = 8 * (j & 7) + (lane >> 3);
    const int ch = ((lane & 7) ^ ((r >> 1) & 7)) << 3;
    if (j < 8) src[i] = (g ? p.Wdi : p.Wdr) + (size_t)(eb * 64 + r) * 512 + ch;
    else src[i] = (const u16*)(p.ws + WS_PQ) + (size_t)(tb * 64 + r) * 1024 + g * 512 + ch;
  }
  auto issue = [&](int st) {
    char* b = lds + (st & 3) * 32768 + w * 4096;
#pragma unroll
    for (int i = 0; i < 4; ++i) glds16(src[i] + st * 64, b + i * 1024);
  };
  const int h = lane >> 5, r32 = lane & 31, sw = (r32 >> 1) & 7;
  int aA[4], aB[4];
#pragma unroll
  for (int ks = 0; ks < 4; ++ks) {
    aA[ks] = g * 16384 + (32 * we + r32) * 128 + (((2 * ks + h) ^ sw) << 4);
    aB[ks] = g * 16384 + 8192 + (32 * wt + r32) * 128 + (((2 * ks + h) ^ sw) << 4);
  }
  s16x8 Af[2][4], Bf[2][4];
  auto rdfr = [&](int set, int st) {
    const char* b = lds + (st & 3) * 32768;
#pragma unroll
    for (int ks = 0; ks < 4; ++ks) { Af[set][ks] = *(const s16x8*)(b + aA[ks]); Bf[set][ks] = *(const s16x8*)(b + aB[ks]); }
  };
  f32x16 acc = {};
  auto mma = [&](int set) {
#pragma unroll
    for (int ks = 0; ks < 4; ++ks) acc = mfma32(Af[set][ks], Bf[set][ks], acc);
  };
  issue(0); issue(1); issue(2); issue(3);
  WAIT_V(12); BAR();
  rdfr(0, 0); WAIT_L0();
#pragma unroll
  for (int u = 0; u < 4; ++u) {
    WAIT_V(8); BAR(); SCHED();
    issue(u + 4);
    rdfr((u + 1) & 1, u + 1);
    mma(u & 1);
    WAIT_L0(); SCHED();
  }
  WAIT_V(8); BAR(); SCHED(); rdfr(1, 5); mma(0); WAIT_L0(); SCHED();
  WAIT_V(4); BAR(); SCHED(); rdfr(0, 6); mma(1); WAIT_L0(); SCHED();
  WAIT_V(0); BAR(); SCHED(); rdfr(1, 7); mma(0); WAIT_L0(); SCHED();
  mma(1);
  BAR();
  {
    float* ex = (float*)lds + (size_t)w * 512;
#pragma unroll
    for (int r4 = 0; r4 < 2; ++r4) {
      f32x4 v = g ? f32x4{acc[4 * r4], acc[4 * r4 + 1], acc[4 * r4 + 2], acc[4 * r4 + 3]}
                  : f32x4{acc[8 + 4 * r4], acc[8 + 4 * r4 + 1], acc[8 + 4 * r4 + 2], acc[8 + 4 * r4 + 3]};
      *(f32x4*)(ex + r4 * 256 + lane * 4) = v;
    }
  }
  __syncthreads();
  {
    const float* ex = (const float*)lds + (size_t)(w ^ 4) * 512;
    char* stg = lds + 65536;
#pragma unroll
    for (int r4 = 0; r4 < 2; ++r4) {
      f32x4 v = *(const f32x4*)(ex + r4 * 256 + lane * 4);
      float o[4];
#pragma unroll
      for (int e = 0; e < 4; ++e) o[e] = g ? (v[e] - acc[8 + 4 * r4 + e]) : (acc[4 * r4 + e] - v[e]);
      const int t = 32 * wt + r32;
      const int e0 = 32 * we + 8 * (2 * g + r4) + 4 * h;
      u32x2 ov = {pk2(o[0], o[1]), pk2(o[2], o[3])};
      *(u32x2*)(stg + t * 128 + (((e0 >> 3) ^ (t & 7)) << 4) + (e0 & 4) * 2) = ov;
    }
  }
  __syncthreads();
  {
    const char* stg = lds + 65536;
    const int t = tid >> 3, c = tid & 7;
    u32x4 v = *(const u32x4*)(stg + t * 128 + ((c ^ (t & 7)) << 4));
    *(u32x4*)(p.out + (size_t)(tb * 64 + t) * 1024 + 512 + eb * 64 + c * 8) = v;
  }
}

DI void softmax_pair(f32x4 pq, s16x4& pos, s16x4& neg) {
  {
    unsigned a = pk2(pq[0], pq[1]), b = pk2(pq[2], pq[3]);
    pq[0] = __uint_as_float(a << 16); pq[1] = __uint_as_float(a & 0xffff0000u);
    pq[2] = __uint_as_float(b << 16); pq[3] = __uint_as_float(b & 0xffff0000u);
  }
  const float L2E = 1.4426950408889634f;
  float mx = xmax4(fmaxf(fmaxf(pq[0], pq[1]), fmaxf(pq[2], pq[3])));
  float mn = xmin4(fminf(fminf(pq[0], pq[1]), fminf(pq[2], pq[3])));
  float ep[4], en[4];
#pragma unroll
  for (int r = 0; r < 4; ++r) { ep[r] = __builtin_amdgcn_exp2f((pq[r] - mx) * L2E); en[r] = __builtin_amdgcn_exp2f((mn - pq[r]) * L2E); }
  float sp = xsum4((ep[0] + ep[1]) + (ep[2] + ep[3]));
  float sn = xsum4((en[0] + en[1]) + (en[2] + en[3]));
  float rp = __builtin_amdgcn_rcpf(sp), rn = __builtin_amdgcn_rcpf(sn);
  pos = pk4(ep[0] * rp, ep[1] * rp, ep[2] * rp, ep[3] * rp);
  neg = pk4(en[0] * rn, en[1] * rn, en[2] * rn, en[3] * rn);
}

DI void a1(const Params& p, int rb, int cb, char* lds) {
  const int tid = threadIdx.x, lane = tid & 63, w = __builtin_amdgcn_readfirstlane(tid >> 6);
  const int wm = w >> 2, hh = w & 3;
  const int head = cb * 4 + hh;
  const u16* src[5]; int sstep[5];
#pragma unroll
  for (int i = 0; i < 5; ++i) {
    const int j = w * 5 + i;
    if (j < 16) {
      const int r = 8 * j + (lane >> 3);
      src[i] = p.Ax + (size_t)(rb * 128 + r) * 512 + (((lane & 7) ^ ((r >> 1) & 7)) << 3);
      sstep[i] = 64;
    } else {
      const int jj = j - 16, sub = jj >> 3;
      const int k = 8 * (jj & 7) + (lane >> 3);
      src[i] = p.Wqkv + (size_t)k * 1536 + sub * 512 + cb * 64 + (((lane & 7) ^ (4 * ((k >> 1) & 1) + 2 * ((k >> 3) & 1))) << 3);
      sstep[i] = 64 * 1536;
    }
  }
  auto issue = [&](int st) {
    char* b = lds + (st % 3) * 40960 + w * 5120;
#pragma unroll
    for (int i = 0; i < 5; ++i) glds16(src[i] + (size_t)st * sstep[i], b + i * 1024);
  };
  const int g = lane >> 4, r16 = lane & 15;
  int aXr[2];
#pragma unroll
  for (int k32 = 0; k32 < 2; ++k32) aXr[k32] = (64 * wm + r16) * 128 + (((4 * k32 + g) ^ ((r16 >> 1) & 7)) << 4);
  const int q = (lane & 15) >> 2, pp = lane & 3;
  const int aWt = 16384 + (8 * g + q) * 128 + (((2 * hh + (pp >> 1)) ^ (4 * ((q >> 1) & 1) + 2 * (g & 1))) << 4) + 8 * (pp & 1);
  s16x8 Xf[2][4][2], Wf[2][3][2];
  auto rdfr = [&](int set, int st) {
    const char* b = lds + (st % 3) * 40960;
#pragma unroll
    for (int k32 = 0; k32 < 2; ++k32) {
#pragma unroll
      for (int s4 = 0; s4 < 4; ++s4) Xf[set][s4][k32] = *(const s16x8*)(b + aXr[k32] + s4 * 2048);
#pragma unroll
      for (int m = 0; m < 3; ++m) Wf[set][m][k32] = cat(trd(b + aWt + m * 8192 + k32 * 4096), trd(b + aWt + m * 8192 + k32 * 4096 + 512));
    }
  };
  f32x4 aQ[4] = {}, aK[4] = {}, aV[4] = {};
  auto mma = [&](int set) {
#pragma unroll
    for (int k32 = 0; k32 < 2; ++k32)
#pragma unroll
      for (int s4 = 0; s4 < 4; ++s4) {
        aQ[s4] = mfma16(Wf[set][0][k32], Xf[set][s4][k32], aQ[s4]);
        aK[s4] = mfma16(Wf[set][1][k32], Xf[set][s4][k32], aK[s4]);
        aV[s4] = mfma16(Xf[set][s4][k32], Wf[set][2][k32], aV[s4]);
      }
  };
  s16x4 wph;
#pragma unroll
  for (int j = 0; j < 4; ++j) wph[j] = (short)p.Wphi[head * 256 + (4 * g + j) * 16 + r16];
  issue(0); issue(1); issue(2);
  WAIT_V(10); BAR();
  rdfr(0, 0); WAIT_L0();
#pragma unroll
  for (int i = 0; i < 5; ++i) {
    WAIT_V(5); BAR(); SCHED();
    issue(i + 3);
    rdfr((i + 1) & 1, i + 1);
    mma(i & 1);
    WAIT_L0(); SCHED();
  }
  WAIT_V(5); BAR(); SCHED(); rdfr(0, 6); mma(1); WAIT_L0(); SCHED();
  WAIT_V(0); BAR(); SCHED(); rdfr(1, 7); mma(0); WAIT_L0(); SCHED();
  mma(1);
  s16x4 ident;
#pragma unroll
  for (int j = 0; j < 4; ++j) ident[j] = (4 * g + j == r16) ? (short)0x3F80 : (short)0;
  const s16x4 ones = {(short)0x3F80, (short)0x3F80, (short)0x3F80, (short)0x3F80};
  f32x4 kvp = {}, kvn = {}, zp = {}, zn = {};
  u16* phiq = (u16*)(p.ws + WS_PHIQ);
#pragma unroll
  for (int s4 = 0; s4 < 4; ++s4) {
    const f32x4 zero = {};
    {
      s16x4 qb = pk4(aQ[s4][0], aQ[s4][1], aQ[s4][2], aQ[s4][3]);
      f32x4 pq = mfma16k16(wph, qb, zero);
      s16x4 pos, neg; softmax_pair(pq, pos, neg);
      const int stile = rb * 8 + wm * 4 + s4;
      *(s16x8*)(phiq + ((size_t)(stile * 32 + head) * 64 + lane) * 8) = cat(pos, neg);
    }
    {
      s16x4 kb = pk4(aK[s4][0], aK[s4][1], aK[s4][2], aK[s4][3]);
      f32x4 pk = mfma16k16(wph, kb, zero);
      s16x4 pos, neg; softmax_pair(pk, pos, neg);
      f32x4 tp = mfma16k16(pos, ident, zero), tn = mfma16k16(neg, ident, zero);
      s16x4 tpb = pk4(tp[0], tp[1], tp[2], tp[3]), tnb = pk4(tn[0], tn[1], tn[2], tn[3]);
      s16x4 vb = pk4(aV[s4][0], aV[s4][1], aV[s4][2], aV[s4][3]);
      kvp = mfma16k16(tpb, vb, kvp); kvn = mfma16k16(tnb, vb, kvn);
      zp = mfma16k16(tpb, ones, zp); zn = mfma16k16(tnb, ones, zn);
    }
  }
  __syncthreads();
  float* ex = (float*)lds;
  if (wm == 1) {
    *(f32x4*)(ex + (hh * 4 + 0) * 256 + lane * 4) = kvp; *(f32x4*)(ex + (hh * 4 + 1) * 256 + lane * 4) = kvn;
    *(f32x4*)(ex + (hh * 4 + 2) * 256 + lane * 4) = zp;  *(f32x4*)(ex + (hh * 4 + 3) * 256 + lane * 4) = zn;
  }
  __syncthreads();
  if (wm == 0) {
    kvp += *(const f32x4*)(ex + (hh * 4 + 0) * 256 + lane * 4); kvn += *(const f32x4*)(ex + (hh * 4 + 1) * 256 + lane * 4);
    zp += *(const f32x4*)(ex + (hh * 4 + 2) * 256 + lane * 4);  zn += *(const f32x4*)(ex + (hh * 4 + 3) * 256 + lane * 4);
    float* kd = (float*)(p.ws + WS_KVP) + ((size_t)(rb * 32 + head) * 64 + lane) * 8;
    *(f32x4*)kd = kvp; *(f32x4*)(kd + 4) = kvn;
    if (r16 == 0) {
      float* zd = (float*)(p.ws + WS_ZP) + ((size_t)(rb * 32 + head) * 4 + g) * 8;
      *(f32x4*)zd = zp; *(f32x4*)(zd + 4) = zn;
    }
  }
}

DI void kvreduce(const Params& p, int j) {
  const int tid = threadIdx.x;
  {
    const int idx = j * 512 + tid;
    const float* src = (const float*)(p.ws + WS_KVP) + (size_t)idx * 4;
    f32x4 s = {};
#pragma unroll
    for (int rb = 0; rb < 16; ++rb) s += *(const f32x4*)(src + (size_t)rb * 32 * 64 * 8);
    *(s16x4*)((u16*)(p.ws + WS_KVF) + (size_t)idx * 4) = pk4(s[0], s[1], s[2], s[3]);
  }
  if (tid < 32) {
    const int idx = j * 32 + tid;
    const float* src = (const float*)(p.ws + WS_ZP) + (size_t)idx * 4;
    f32x4 s = {};
#pragma unroll
    for (int rb = 0; rb < 16; ++rb) s += *(const f32x4*)(src + (size_t)rb * 32 * 4 * 8);
    *(s16x4*)((u16*)(p.ws + WS_ZF) + (size_t)idx * 4) = pk4(s[0], s[1], s[2], s[3]);
  }
}

DI float rbf(float x) { return __uint_as_float(pk2(x, x) << 16); }
DI void a3(const Params& p, int sb, int nb, char* lds) {
  const int tid = threadIdx.x, lane = tid & 63, w = __builtin_amdgcn_readfirstlane(tid >> 6);
  const int s4 = w >> 1, nh = w & 1;
#pragma unroll
  for (int i = 0; i < 16; ++i) {
    const int j = w * 16 + i;
    const int kb = j >> 4, sub = (j >> 3) & 1;
    const int k = 8 * (j & 7) + (lane >> 3);
    glds16(p.Wo + (size_t)(kb * 64 + k) * 512 + nb * 128 + sub * 64 + (((lane & 7) ^ (4 * ((k >> 1) & 1) + 2 * ((k >> 2) & 1))) << 3), lds + j * 1024);
  }
  const int g = lane >> 4, r16 = lane & 15, q = r16 >> 2, pp = lane & 3;
  const int stile = sb * 4 + s4;
  const u16* phiq = (const u16*)(p.ws + WS_PHIQ) + ((size_t)stile * 32 * 64 + lane) * 8;
  const u16* kvf = (const u16*)(p.ws + WS_KVF) + lane * 8;
  const u16* zf = (const u16*)(p.ws + WS_ZF) + g * 8;
  s16x8 of[16];
#pragma unroll
  for (int hp = 0; hp < 16; ++hp) {
    s16x4 o2[2];
#pragma unroll
    for (int e = 0; e < 2; ++e) {
      const int h = 2 * hp + e;
      s16x8 ph = *(const s16x8*)(phiq + (size_t)h * 512);
      s16x8 kf = *(const s16x8*)(kvf + (size_t)h * 512);
      s16x8 zz = *(const s16x8*)(zf + h * 32);
      const f32x4 zero = {};
      f32x4 num = mfma16(kf, ph, zero), den = mfma16(zz, ph, zero);
      float dn = rbf(rbf(den[0]) + 1e-6f);
      float r = 1.0f / dn;
      o2[e] = pk4(rbf(num[0]) * r, rbf(num[1]) * r, rbf(num[2]) * r, rbf(num[3]) * r);
    }
    of[hp] = cat(o2[0], o2[1]);
  }
  WAIT_V(0); __syncthreads();
  f32x4 acc[4] = {};
  const int krow = 4 * g + q;
  const int swz = 4 * ((krow >> 1) & 1) + 2 * ((krow >> 2) & 1);
#pragma unroll
  for (int hp = 0; hp < 16; ++hp) {
#pragma unroll
    for (int nt = 0; nt < 4; ++nt) {
      const char* b = lds + (hp >> 1) * 16384 + nh * 8192 + (32 * (hp & 1) + krow) * 128 + (((2 * nt + (pp >> 1)) ^ swz) << 4) + 8 * (pp & 1);
      s16x8 wf = cat(trd(b), trd(b + 2048));
      acc[nt] = mfma16(wf, of[hp], acc[nt]);
    }
  }
  u16* dst = p.out + (size_t)(sb * 64 + s4 * 16 + r16) * 1024 + nb * 128 + nh * 64 + 4 * g;
#pragma unroll
  for (int nt = 0; nt < 4; ++nt) *(s16x4*)(dst + nt * 16) = pk4(acc[nt][0], acc[nt][1], acc[nt][2], acc[nt][3]);
}

__global__ __launch_bounds__(512, 2) void k1(Params p) {
  extern __shared__ __attribute__((aligned(16))) char lds[];
  const int b = blockIdx.x, xcd = b & 7, i = b >> 3;
  if (i < 16) {
    bbig(p, 2 * xcd + (i >> 3), (i >> 2) & 1, i & 3, lds);
  } else {
    const int a = i - 16;
    a1(p, 2 * xcd + (a >> 3), a & 7, lds);
  }
}
__global__ __launch_bounds__(512, 2) void k2(Params p) {
  extern __shared__ __attribute__((aligned(16))) char lds[];
  const int b = blockIdx.x;
  if (b < 256) { const int xcd = b & 7, i = b >> 3; bsmall(p, 4 * xcd + (i >> 3), i & 7, lds); }
  else kvreduce(p, b - 256);
}
__global__ __launch_bounds__(512, 2) void k3(Params p) {
  extern __shared__ __attribute__((aligned(16))) char lds[];
  const int b = blockIdx.x, xcd = b & 7, i = b >> 3;
  a3(p, 4 * xcd + (i >> 2), i & 3, lds);
}

extern "C" void kernel_launch(void* const* d_in, const int* in_sizes, int n_in, void* d_out, int out_size, void* d_ws, size_t ws_size, hipStream_t stream) {
  Params p;
  p.Ax = (const u16*)d_in[0]; p.Wqkv = (const u16*)d_in[1]; p.Wphi = (const u16*)d_in[2]; p.Wo = (const u16*)d_in[3];
  p.Bx = (const u16*)d_in[4]; p.Wsr = (const u16*)d_in[5]; p.Wsi = (const u16*)d_in[6]; p.Wdr = (const u16*)d_in[7]; p.Wdi = (const u16*)d_in[8];
  p.out = (u16*)d_out; p.ws = (char*)d_ws;
  hipFuncSetAttribute((const void*)k1, hipFuncAttributeMaxDynamicSharedMemorySize, 131072);
  hipFuncSetAttribute((const void*)k2, hipFuncAttributeMaxDynamicSharedMemorySize, 131072);
  hipFuncSetAttribute((const void*)k3, hipFuncAttributeMaxDynamicSharedMemorySize, 131072);
  k1<<<256, 512, 131072, stream>>>(p);
  k2<<<264, 512, 131072, stream>>>(p);
  k3<<<128, 512, 131072, stream>>>(p);
}
```

```cpp
#include <hip/hip_runtime.h>
#include <hip/hip_bf16.h>
#include <stdint.h>

typedef unsigned short u16;
typedef unsigned long long u64;
typedef __attribute__((ext_vector_type(8))) short s16x8;
typedef __attribute__((ext_vector_type(4))) short s16x4;
typedef __attribute__((ext_vector_type(8))) __bf16 bf16x8;
typedef __attribute__((ext_vector_type(2))) __bf16 bf16x2;
typedef __attribute__((ext_vector_type(16))) float f32x16;
typedef __attribute__((ext_vector_type(4))) float f32x4;
typedef __attribute__((ext_vector_type(2))) float f32x2;
typedef __attribute__((ext_vector_type(4))) unsigned u32x4;
typedef __attribute__((ext_vector_type(2))) unsigned u32x2;
#define LAS __attribute__((address_space(3)))
#define GAS __attribute__((address_space(1)))
#define DI __device__ __forceinline__
#define RLX_AGENT __ATOMIC_RELAXED, __HIP_MEMORY_SCOPE_AGENT

#define WAIT_V(n) asm volatile("s_waitcnt vmcnt(%0)" :: "i"(n) : "memory")
#define WAIT_L0() asm volatile("s_waitcnt lgkmcnt(0)" ::: "memory")
#define BAR() __builtin_amdgcn_s_barrier()
#define SCHED() __builtin_amdgcn_sched_barrier(0)

DI f32x16 mfma32(s16x8 a, s16x8 b, f32x16 c) { return __builtin_amdgcn_mfma_f32_32x32x16_bf16(__builtin_bit_cast(bf16x8, a), __builtin_bit_cast(bf16x8, b), c, 0, 0, 0); }
DI f32x4 mfma16(s16x8 a, s16x8 b, f32x4 c) { return __builtin_amdgcn_mfma_f32_16x16x32_bf16(__builtin_bit_cast(bf16x8, a), __builtin_bit_cast(bf16x8, b), c, 0, 0, 0); }
DI f32x4 mfma16k16(s16x4 a, s16x4 b, f32x4 c) { return __builtin_amdgcn_mfma_f32_16x16x16bf16_1k(a, b, c, 0, 0, 0); }
DI unsigned pk2(float a, float b) { f32x2 p = {a, b}; bf16x2 q = __builtin_convertvector(p, bf16x2); return __builtin_bit_cast(unsigned, q); }
DI s16x4 pk4(float a, float b, float c, float d) { u32x2 r = {pk2(a, b), pk2(c, d)}; return __builtin_bit_cast(s16x4, r); }
DI float rbf(float x) { return __uint_as_float(pk2(x, x) << 16); }
DI s16x4 trd(unsigned a, int off) { s16x4 r; asm volatile("ds_read_b64_tr_b16 %0, %1 offset:%2" : "=v"(r) : "v"(a), "i"(off) : "memory"); return r; }
DI s16x8 rd128(unsigned a, int off) { s16x8 r; asm volatile("ds_read_b128 %0, %1 offset:%2" : "=v"(r) : "v"(a), "i"(off) : "memory"); return r; }
DI unsigned ldsaddr(const char* p) { return (unsigned)(size_t)(LAS const char*)p; }
DI s16x8 cat(s16x4 a, s16x4 b) { s16x8 r; r.lo = a; r.hi = b; return r; }
DI void glds16(const void* g, char* l) { __builtin_amdgcn_global_load_lds((const unsigned*)g, (unsigned*)l, 16, 0, 0); }
#define OUTA_AUX 16
#define OUTB_AUX 16
#define PF_AUX 2
#define WS_AUX 0
DI void glds16nt(const void* g, char* l) { __builtin_amdgcn_global_load_lds((const unsigned*)g, (unsigned*)l, 16, 0, WS_AUX); }
#define APF_AUX 0
#define A1X_AUX 0
#define A1ROT ((cb + rb) & 7)
#define A1_AUX 0
#define APFS 9
#define APF0 3
#define APF1 5
#define PFD 10
DI void touch_nt(const void* g, unsigned& dummy) { asm volatile("global_load_dword %0, %1, off nt" : "+v"(dummy) : "v"(g) : "memory"); }
DI void touch(const void* g, unsigned& dummy) { asm volatile("global_load_dword %0, %1, off" : "+v"(dummy) : "v"(g) : "memory"); }
DI float vmax(float a, float b) { float r; asm("v_max_f32 %0, %1, %2" : "=v"(r) : "v"(a), "v"(b)); return r; }
DI float vmin(float a, float b) { float r; asm("v_min_f32 %0, %1, %2" : "=v"(r) : "v"(a), "v"(b)); return r; }
DI float vmax3(float a, float b, float c) { float r; asm("v_max3_f32 %0, %1, %2, %3" : "=v"(r) : "v"(a), "v"(b), "v"(c)); return r; }
DI float vmin3(float a, float b, float c) { float r; asm("v_min3_f32 %0, %1, %2, %3" : "=v"(r) : "v"(a), "v"(b), "v"(c)); return r; }
DI void swap16(float x, float& a, float& b) { auto s = __builtin_amdgcn_permlane16_swap(__float_as_uint(x), __float_as_uint(x), false, false); a = __uint_as_float(s[0]); b = __uint_as_float(s[1]); }
DI void swap32(float x, float& a, float& b) { auto s = __builtin_amdgcn_permlane32_swap(__float_as_uint(x), __float_as_uint(x), false, false); a = __uint_as_float(s[0]); b = __uint_as_float(s[1]); }
DI float xmax4(float x) {
  auto s = __builtin_amdgcn_permlane16_swap(__float_as_uint(x), __float_as_uint(x), false, false);
  x = fmaxf(__uint_as_float(s[0]), __uint_as_float(s[1]));
  auto t = __builtin_amdgcn_permlane32_swap(__float_as_uint(x), __float_as_uint(x), false, false);
  return fmaxf(__uint_as_float(t[0]), __uint_as_float(t[1]));
}
DI float xmin4(float x) {
  auto s = __builtin_amdgcn_permlane16_swap(__float_as_uint(x), __float_as_uint(x), false, false);
  x = fminf(__uint_as_float(s[0]), __uint_as_float(s[1]));
  auto t = __builtin_amdgcn_permlane32_swap(__float_as_uint(x), __float_as_uint(x), false, false);
  return fminf(__uint_as_float(t[0]), __uint_as_float(t[1]));
}
DI float xsum4(float x) {
  auto s = __builtin_amdgcn_permlane16_swap(__float_as_uint(x), __float_as_uint(x), false, false);
  x = __uint_as_float(s[0]) + __uint_as_float(s[1]);
  auto t = __builtin_amdgcn_permlane32_swap(__float_as_uint(x), __float_as_uint(x), false, false);
  return __uint_as_float(t[0]) + __uint_as_float(t[1]);
}

constexpr unsigned WS_PQF = 0;
constexpr unsigned WS_PHIQ = 4u << 20;
constexpr unsigned WS_KVP = 8u << 20;
constexpr unsigned WS_ZP = 9u << 20;
constexpr unsigned WS_KVF = 10u << 20;
constexpr unsigned WS_ZF = WS_KVF + 32768;
constexpr unsigned WS_KVPG = 12u << 20;
constexpr unsigned WS_ZPG = 14u << 20;
constexpr unsigned WS_KVFG = (14u << 20) + 262144;
constexpr unsigned WS_ZFG = WS_KVFG + 65536;
constexpr unsigned WS_SYNC = 11u << 20;
constexpr unsigned SY_EPOCH = WS_SYNC;
constexpr unsigned SY_START = WS_SYNC + 4096;
constexpr unsigned SY_PQ = WS_SYNC + 8192;
constexpr unsigned SY_A1 = WS_SYNC + 12288;
constexpr unsigned SY_A1T = WS_SYNC + 16384;
constexpr unsigned SY_RED = WS_SYNC + 20480;
constexpr unsigned SY_TMO = WS_SYNC + 24576;
constexpr unsigned WS_BYTES = 16u << 20;
constexpr int LDS_BYTES = 163840;
constexpr int LDS_MISC = 163840 - 64;

struct Params {
  const u16 *Ax, *Wqkv, *Wphi, *Wo, *Bx, *Wsr, *Wsi, *Wdr, *Wdi;
  u16* out; char* ws;
};
typedef __amdgpu_buffer_rsrc_t rsrc_t;
DI rsrc_t mkrs(const void* base, unsigned bytes) { return __builtin_amdgcn_make_buffer_rsrc((void*)base, 0, bytes, 0x00020000); }
template <int AUX> DI void bl16(rsrc_t r, unsigned voff, unsigned soff, char* l) { __builtin_amdgcn_raw_ptr_buffer_load_lds(r, (LAS void*)l, 16, voff, soff, 0, AUX); }
DI u32x2 ld_sc1_64(rsrc_t r, unsigned off) { return __builtin_amdgcn_raw_buffer_load_b64(r, off, 0, 16); }
DI u32x4 ld_sc1(rsrc_t r, unsigned off) { return __builtin_amdgcn_raw_buffer_load_b128(r, off, 0, 16); }
DI u32x4 ld_pl(rsrc_t r, unsigned off) { return __builtin_amdgcn_raw_buffer_load_b128(r, off, 0, 0); }
DI void acquire_agent() { __builtin_amdgcn_fence(__ATOMIC_ACQUIRE, "agent"); asm volatile("s_waitcnt vmcnt(0)" ::: "memory"); }
DI void st_sc1(rsrc_t r, unsigned off, u32x4 v) { __builtin_amdgcn_raw_buffer_store_b128(v, r, off, 0, 16); }
DI void st_sc1_64(rsrc_t r, unsigned off, u32x2 v) { __builtin_amdgcn_raw_buffer_store_b64(v, r, off, 0, 16); }
DI u64 ld_flag(const char* ws, unsigned off) { return __hip_atomic_load((GAS const u64*)(ws + off), RLX_AGENT); }
DI void st_flag(char* ws, unsigned off, u64 v) { __hip_atomic_store((GAS u64*)(ws + off), v, RLX_AGENT); }
#define POLL_SLEEP 4
constexpr unsigned SPIN_CAP = 1u << 18;
DI void wait_flags(char* ws, unsigned base, int n, u64 tag, int lane) {
  for (unsigned spins = 0;; ++spins) {
    u64 v = tag;
    if (lane < n) v = ld_flag(ws, base + lane * 8);
    if (__all(v == tag)) break;
    __builtin_amdgcn_s_sleep(POLL_SLEEP);
    if (spins > SPIN_CAP) { if (lane == 0) st_flag(ws, SY_TMO, tag); break; }
  }
}
DI void wait_flags128(char* ws, unsigned base, u64 tag, int lane) {
  for (unsigned spins = 0;; ++spins) {
    u64 v0 = ld_flag(ws, base + lane * 8), v1 = ld_flag(ws, base + 512 + lane * 8);
    if (__all(v0 == tag && v1 == tag)) break;
    __builtin_amdgcn_s_sleep(POLL_SLEEP);
    if (spins > SPIN_CAP) { if (lane == 0) st_flag(ws, SY_TMO, tag); break; }
  }
}

DI void read_epoch(const Params& p, char* lds) {
  const u64 t = ld_flag(p.ws, SY_EPOCH) + 1;
  *(volatile LAS u64*)(lds + LDS_MISC) = t;
  *(volatile LAS unsigned*)(lds + LDS_MISC + 8) = 0u;
  *(volatile LAS unsigned*)(lds + LDS_MISC + 12) = 0u;
  st_flag(p.ws, SY_START + blockIdx.x * 8, t);
}
DI void bbig(const Params& p, rsrc_t rs, int tb, int ri, int cb, char* lds) {
  const int tid = threadIdx.x, lane = tid & 63, w = __builtin_amdgcn_readfirstlane(tid >> 6);
  const u16* Ws = (ri ? p.Wsi : p.Wsr) + (size_t)tb * 128 * 2048;
  const u16* Xc = p.Bx + cb * 128;
#define ROT (tb * 2 + ri)
  const int rot = (ROT) & 31;
  if (w >= 4) {
    {
      const int lw = w - 4;
      const rsrc_t rW = mkrs(Ws, 128 * 4096), rX = mkrs(p.Bx, 2048 * 1024);
      unsigned vW[4], vX[4];
#pragma unroll
      for (int i = 0; i < 4; ++i) {
        const int j = 4 * lw + i;
        const int r = 8 * j + (lane >> 3);
        vW[i] = r * 4096 + (((lane & 7) ^ ((r >> 1) & 7)) << 4);
        const int k = 8 * (j & 7) + (lane >> 3);
        vX[i] = k * 1024 + cb * 256 + (j >> 3) * 128 + (((lane & 7) ^ (4 * ((k >> 1) & 1) + 2 * ((k >> 3) & 1))) << 4);
      }
      auto issue = [&](int st) {
        char* b = lds + (st & 3) * 32768 + lw * 4096;
        const int gs = (st + rot) & 31;
        const int gw = gs;
        const int gx = gs;
#pragma unroll
        for (int i = 0; i < 4; ++i) { bl16<WS_AUX>(rW, vW[i], gw * 128, b + i * 1024); bl16<0>(rX, vX[i], gx * 65536, b + 16384 + i * 1024); }
      };
      issue(0); issue(1); issue(2);
      WAIT_V(16); BAR();
      WAIT_V(8); BAR();
#pragma unroll 1
      for (int i = 0; i < 29; ++i) { issue(i + 3); WAIT_V(8); BAR(); }
      WAIT_V(0); BAR();
      BAR();
      BAR();
    }
  } else {
    f32x16 acc[2][2] = {};
    const int wt = (w >> 1) & 1, wd = w & 1;
    const int h = lane >> 5, r32 = lane & 31;
    const int sw = (r32 >> 1) & 7;
    const unsigned lb = ldsaddr(lds);
    unsigned bW[2][4], bX[2][2];
    const int blk = (lane >> 4) & 1, q = (lane & 15) >> 2, pp = lane & 3;
#pragma unroll
    for (int hf = 0; hf < 2; ++hf) {
#pragma unroll
      for (int ks = 0; ks < 4; ++ks) bW[hf][ks] = lb + hf * 65536 + (64 * wt + r32) * 128 + (((2 * ks + h) ^ sw) << 4);
#pragma unroll
      for (int di = 0; di < 2; ++di)
        bX[hf][di] = lb + hf * 65536 + 16384 + wd * 8192 + (8 * h + q) * 128 + (((4 * di + 2 * blk + (pp >> 1)) ^ (4 * ((q >> 1) & 1) + 2 * h)) << 4) + 8 * (pp & 1);
    }
    const rsrc_t rWp = mkrs(Ws, 128 * 4096), rXp = mkrs(p.Bx, 2048 * 1024);
    const unsigned vWp = (32 * cb + 8 * w + (lane >> 3)) * 4096 + ((lane & 7) << 4);
    const int wi = ((((tb & 1) * 2 + ri) * 4 + cb) * 4 + w);
    const unsigned vXp = wi * 1024 + lane * 16;
    const int rotx = (tb >> 1) * 4 + 3;
    char* dm = lds + 131072 + w * 2048;
    auto pf = [&](int st) {
      bl16<0>(rXp, vXp, ((st + rotx) & 31) * 65536, dm + 1024);
    };
    s16x8 Wf[2][2][2]; s16x4 Xl[2][2][2], Xh[2][2][2];
    auto rdgap = [&](int hsel, int st, int g) {
      const int hf = (st >> 1) & 1, o = (st & 1) * 32768;
      const int ks2 = g >> 1, i = g & 1, ks = hsel * 2 + ks2;
      Wf[hsel][i][ks2] = rd128(bW[hf][ks], o + i * 4096);
      Xl[hsel][i][ks2] = trd(bX[hf][i], o + ks * 2048);
      Xh[hsel][i][ks2] = trd(bX[hf][i], o + ks * 2048 + 512);
    };
    auto half = [&](int hc, bool rd, int hn, int stn) {
#pragma unroll
      for (int m = 0; m < 8; ++m) {
        const int ks2 = m >> 2, di = (m >> 1) & 1, tj = m & 1;
        acc[di][tj] = mfma32(cat(Xl[hc][di][ks2], Xh[hc][di][ks2]), Wf[hc][tj][ks2], acc[di][tj]);
        SCHED();
        if (rd && m < 4) { rdgap(hn, stn, m); SCHED(); }
      }
      WAIT_L0(); SCHED();
    };
    if (tid == 0) read_epoch(p, lds);
    BAR();
#pragma unroll 1
    for (int c = 3; c < APFS; ++c) bl16<0>(rWp, vWp, ((c + rot) & 31) * 128, dm);
#pragma unroll 1
    for (int c = 3; c < PFD; ++c) pf(c);
#pragma unroll
    for (int g = 0; g < 4; ++g) rdgap(0, 0, g);
    WAIT_L0(); SCHED();
    BAR();
#pragma unroll 1
    for (int i = 0; i < 28; i += 4) {
#pragma unroll
      for (int u = 0; u < 4; ++u) { if (i + u + PFD < 32) pf(i + u + PFD); half(0, true, 1, u); half(1, true, 0, u + 1); BAR(); SCHED();
      }
    }
#pragma unroll
    for (int u = 0; u < 3; ++u) { half(0, true, 1, u); half(1, true, 0, u + 1); BAR(); SCHED(); }
    half(0, true, 1, 3); half(1, false, 0, 0);
    BAR();
#pragma unroll
    for (int tj = 0; tj < 2; ++tj) {
      const int ttile = tb * 4 + 2 * wt + tj;
#pragma unroll
      for (int di = 0; di < 2; ++di)
#pragma unroll
        for (int pr = 0; pr < 2; ++pr) {
          const f32x16& a = acc[di][tj];
          unsigned ax = pk2(a[8 * pr], a[8 * pr + 1]), ay = pk2(a[8 * pr + 2], a[8 * pr + 3]);
          unsigned bx = pk2(a[8 * pr + 4], a[8 * pr + 5]), by = pk2(a[8 * pr + 6], a[8 * pr + 7]);
          auto r0 = __builtin_amdgcn_permlane32_swap(ax, bx, false, false);
          auto r1 = __builtin_amdgcn_permlane32_swap(ay, by, false, false);
          const int kap = ri * 32 + cb * 8 + wd * 4 + di * 2 + pr;
          st_sc1(rs, WS_PQF + (unsigned)((ttile * 64 + kap) * 64 + lane) * 16, u32x4{r0[0], r1[0], r0[1], r1[1]});
        }
    }
    WAIT_V(0);
  }
}

DI void bsmall_prefetch(const Params& p, int eb, char* lds) {
  const int lane = threadIdx.x & 63, w = __builtin_amdgcn_readfirstlane(threadIdx.x >> 6);
#pragma unroll
  for (int i = 0; i < 16; ++i) {
    const int j = w * 16 + i;
    const int tile = j >> 3, r = 8 * (j & 7) + (lane >> 3);
    const u16* src = ((tile >> 3) ? p.Wdi : p.Wdr) + (size_t)(eb * 64 + r) * 512 + (tile & 7) * 64 + (((lane & 7) ^ ((r >> 1) & 7)) << 3);
    glds16(src, lds + j * 1024);
  }
}
DI void bsmall(const Params& p, rsrc_t rs, int tb64, int eb, u64 tag, char* lds) {
  const int tid = threadIdx.x, lane = tid & 63, w = __builtin_amdgcn_readfirstlane(tid >> 6);
  const int kq = w >> 1, wt = w & 1;
  if (w == 0) {
    for (unsigned spins = 0;; ++spins) {
      u64 v = tag;
      if (lane < 16) v = ld_flag(p.ws, SY_PQ + ((tb64 >> 1) + (lane >> 3)) * 64 + (lane & 7) * 8);
      if (__all(v == tag)) break;
      __builtin_amdgcn_s_sleep(POLL_SLEEP);
      if (spins > SPIN_CAP) { if (lane == 0) st_flag(p.ws, SY_TMO, tag); break; }
    }
  }
  WAIT_V(0);
  BAR();
  u32x4 pf[2][16];
#pragma unroll
  for (int tt = 0; tt < 2; ++tt) {
    const unsigned pbase = WS_PQF + (unsigned)((((tb64 + 2 * tt) * 2 + wt) * 64 + kq * 16) * 64 + lane) * 16;
#pragma unroll
    for (int i = 0; i < 16; ++i) {
      pf[tt][i] = ld_sc1(rs, pbase + ((((i >> 2) + eb) & 3) * 4 + (i & 3)) * 1024);
    }
  }
  const int h = lane >> 5, r32 = lane & 31, sw = (r32 >> 1) & 7;
  f32x16 acc[2][2] = {};
  const unsigned lb = ldsaddr(lds) + kq * 32768 + r32 * 128;
  unsigned aA[4];
#pragma unroll
  for (int ks = 0; ks < 4; ++ks) aA[ks] = lb + (((2 * ks + h) ^ sw) << 4);
#pragma unroll
  for (int kb = 0; kb < 4; ++kb) {
    s16x8 Af[4][2];
#pragma unroll
    for (int ks = 0; ks < 4; ++ks)
#pragma unroll
      for (int ei = 0; ei < 2; ++ei) Af[ks][ei] = rd128(aA[ks] + ((kb + eb) & 3) * 8192, ei * 4096);
    WAIT_L0(); SCHED();
#pragma unroll
    for (int tt = 0; tt < 2; ++tt)
#pragma unroll
      for (int ks = 0; ks < 4; ++ks)
#pragma unroll
        for (int ei = 0; ei < 2; ++ei) acc[tt][ei] = mfma32(Af[ks][ei], __builtin_bit_cast(s16x8, pf[tt][kb * 4 + ks]), acc[tt][ei]);
    SCHED();
  }
  float* ex = (float*)(lds + 131072);
#pragma unroll
  for (int tt = 0; tt < 2; ++tt) {
    float o[8];
#pragma unroll
    for (int ei = 0; ei < 2; ++ei) {
      BAR();
#pragma unroll
      for (int r4 = 0; r4 < 4; ++r4)
        *(f32x4*)(ex + ((w * 4 + r4) * 64 + lane) * 4) = f32x4{acc[tt][ei][4 * r4], acc[tt][ei][4 * r4 + 1], acc[tt][ei][4 * r4 + 2], acc[tt][ei][4 * r4 + 3]};
      WAIT_L0();
      BAR();
      if ((kq >> 1) == ei) {
#pragma unroll
        for (int r4 = 0; r4 < 2; ++r4) {
          f32x4 s = {};
#pragma unroll
          for (int k = 0; k < 4; ++k) {
            f32x4 v = *(const f32x4*)(ex + (((k * 2 + wt) * 4 + (kq & 1) * 2 + r4) * 64 + lane) * 4);
            s = (k < 2) ? s + v : s - v;
          }
#pragma unroll
          for (int e = 0; e < 4; ++e) o[r4 * 4 + e] = s[e];
        }
        WAIT_L0();
      }
    }
    unsigned ax = pk2(o[0], o[1]), ay = pk2(o[2], o[3]), bx = pk2(o[4], o[5]), by = pk2(o[6], o[7]);
    auto r0 = __builtin_amdgcn_permlane32_swap(ax, bx, false, false);
    auto r1 = __builtin_amdgcn_permlane32_swap(ay, by, false, false);
    const int t = (tb64 + 2 * tt) * 64 + wt * 32 + r32;
    const int e0 = eb * 64 + 32 * (kq >> 1) + 16 * (kq & 1) + 8 * h;
    __builtin_amdgcn_raw_buffer_store_b128(u32x4{r0[0], r1[0], r0[1], r1[1]}, mkrs(p.out, 4194304u), (unsigned)(t * 1024 + 512 + e0) * 2u, 0, OUTB_AUX);
  }
}

template <int N>
DI void softmax_n(f32x4 (&pq)[N], s16x4 (&pos)[N], s16x4 (&neg)[N]) {
  const float L2E = 1.4426950408889634f;
  float mx[N], mn[N], a, b;
#pragma unroll
  for (int t = 0; t < N; ++t) {
    unsigned u = pk2(pq[t][0], pq[t][1]), v = pk2(pq[t][2], pq[t][3]);
    pq[t][0] = __uint_as_float(u << 16); pq[t][1] = __uint_as_float(u & 0xffff0000u);
    pq[t][2] = __uint_as_float(v << 16); pq[t][3] = __uint_as_float(v & 0xffff0000u);
  }
#pragma unroll
  for (int t = 0; t < N; ++t) { mx[t] = vmax(vmax3(pq[t][0], pq[t][1], pq[t][2]), pq[t][3]); mn[t] = vmin(vmin3(pq[t][0], pq[t][1], pq[t][2]), pq[t][3]); }
#pragma unroll
  for (int t = 0; t < N; ++t) { swap16(mx[t], a, b); mx[t] = vmax(a, b); swap16(mn[t], a, b); mn[t] = vmin(a, b); }
#pragma unroll
  for (int t = 0; t < N; ++t) { swap32(mx[t], a, b); mx[t] = vmax(a, b); swap32(mn[t], a, b); mn[t] = vmin(a, b); }
  float ep[N][4], en[N][4], sp[N], sn[N];
#pragma unroll
  for (int t = 0; t < N; ++t)
#pragma unroll
    for (int r = 0; r < 4; ++r) { ep[t][r] = __builtin_amdgcn_exp2f((pq[t][r] - mx[t]) * L2E); en[t][r] = __builtin_amdgcn_exp2f((mn[t] - pq[t][r]) * L2E); }
#pragma unroll
  for (int t = 0; t < N; ++t) { sp[t] = (ep[t][0] + ep[t][1]) + (ep[t][2] + ep[t][3]); sn[t] = (en[t][0] + en[t][1]) + (en[t][2] + en[t][3]); }
#pragma unroll
  for (int t = 0; t < N; ++t) { swap16(sp[t], a, b); sp[t] = a + b; swap16(sn[t], a, b); sn[t] = a + b; }
#pragma unroll
  for (int t = 0; t < N; ++t) { swap32(sp[t], a, b); sp[t] = __builtin_amdgcn_rcpf(a + b); swap32(sn[t], a, b); sn[t] = __builtin_amdgcn_rcpf(a + b); }
#pragma unroll
  for (int t = 0; t < N; ++t) {
    pos[t] = pk4(ep[t][0] * sp[t], ep[t][1] * sp[t], ep[t][2] * sp[t], ep[t][3] * sp[t]);
    neg[t] = pk4(en[t][0] * sn[t], en[t][1] * sn[t], en[t][2] * sn[t], en[t][3] * sn[t]);
  }
}

DI void kvreduce(const Params& p, rsrc_t rs, int head, int j, u64 tag) {
  const int lane = threadIdx.x & 63;
  const unsigned t32 = (unsigned)tag ^ (unsigned)(tag >> 32);
  u32x4 v[16], zv;
  for (unsigned spins = 0;; ++spins) {
#pragma unroll
    for (int k = 0; k < 16; ++k) v[k] = ld_sc1(rs, WS_KVPG + (unsigned)(((k * 32 + head) * 4 + j) * 64 + lane) * 16);
    zv = ld_sc1(rs, WS_ZPG + (unsigned)((head * 4 + j) * 64 + lane) * 16);
    bool ok = zv[1] == t32 && zv[3] == t32;
#pragma unroll
    for (int k = 0; k < 16; ++k) ok = ok && v[k][1] == t32 && v[k][3] == t32;
    if (__all(ok)) break;
    __builtin_amdgcn_s_sleep(POLL_SLEEP);
    if (spins > SPIN_CAP) { if (lane == 0) st_flag(p.ws, SY_TMO, tag); break; }
  }
  float a = 0.f, b = 0.f;
#pragma unroll
  for (int k = 0; k < 16; ++k) { a += __uint_as_float(v[k][0]); b += __uint_as_float(v[k][2]); }
  st_sc1_64(rs, WS_KVFG + (unsigned)((head * 4 + j) * 64 + lane) * 8, u32x2{pk2(a, b), t32});
  float za = __uint_as_float(zv[0]), zb = __uint_as_float(zv[2]);
#pragma unroll
  for (int m = 4; m < 64; m <<= 1) { za += __shfl_xor(za, m); zb += __shfl_xor(zb, m); }
  if (lane < 4) st_sc1_64(rs, WS_ZFG + (unsigned)((head * 4 + j) * 4 + lane) * 8, u32x2{pk2(za, zb), t32});
}

DI void a3_prefetch(const Params& p, int nb, char* lds);
DI void a1(const Params& p, rsrc_t rs, int rb, int cb, char* lds) {
  const int tid = threadIdx.x, lane = tid & 63, w = __builtin_amdgcn_readfirstlane(tid >> 6);
  const int wm = w >> 2, hh = w & 3;
  const int head = cb * 4 + hh;
  const rsrc_t rA = mkrs(p.Ax + (size_t)rb * 128 * 512, 128 * 1024), rQ = mkrs(p.Wqkv, 512 * 3072);
  unsigned vo[10];
  const int lw = w & 3;
#pragma unroll
  for (int i = 0; i < 10; ++i) {
    const int j = lw * 10 + i;
    if (j < 16) {
      const int r = 8 * j + (lane >> 3);
      vo[i] = r * 1024 + (((lane & 7) ^ ((r >> 1) & 7)) << 4);
    } else {
      const int jj = j - 16, sub = jj >> 3;
      const int k = 8 * (jj & 7) + (lane >> 3);
      vo[i] = k * 3072 + sub * 1024 + cb * 128 + (((lane & 7) ^ (4 * ((k >> 1) & 1) + 2 * ((k >> 3) & 1))) << 4);
    }
  }
  const int rot = A1ROT;
  auto issue1 = [&](int st, int i) {
    char* b = lds + (st % 3) * 40960 + lw * 10240;
    const int gs = (st + rot) & 7;
    if (lw * 10 + i < 16) bl16<A1X_AUX>(rA, vo[i], gs * 128, b + i * 1024); else bl16<A1_AUX>(rQ, vo[i], gs * 64 * 3072, b + i * 1024);
  };
  auto issue = [&](int st) {
    if (w < 4) {
#pragma unroll
      for (int i = 0; i < 10; ++i) issue1(st, i);
    }
  };
  const int wi = (((rb & 1) * 8 + cb) * 4 + lw);
  const int ptb = (rb & ~1) + (wi >> 5), pri = (wi >> 4) & 1;
  const rsrc_t rP = mkrs((pri ? p.Wsi : p.Wsr) + (size_t)ptb * 128 * 2048, 128 * 4096);
  const unsigned vP = (8 * (wi & 15) + (lane >> 3)) * 4096 + ((lane & 7) << 4);
  const int prot = ptb * 2 + pri;
  auto pf1 = [&](int st) { if (st < 32) bl16<APF_AUX>(rP, vP, ((st + prot) & 31) * 128, lds + 131072 + lw * 1024); };
  auto pf = [&](int st0, int n) {
    if (w >= 4) {
      char* dm = lds + 131072 + lw * 1024;
#pragma unroll 1
      for (int st = st0; st < st0 + n; ++st) if (st < 32) bl16<APF_AUX>(rP, vP, ((st + prot) & 31) * 128, dm);
    }
  };
  const int g = lane >> 4, r16 = lane & 15;
  int aXr[2];
#pragma unroll
  for (int k32 = 0; k32 < 2; ++k32) aXr[k32] = (64 * wm + r16) * 128 + (((4 * k32 + g) ^ ((r16 >> 1) & 7)) << 4);
  const int q = (lane & 15) >> 2, pp = lane & 3;
  const int aWt = 16384 + (8 * g + q) * 128 + (((2 * hh + (pp >> 1)) ^ (4 * ((q >> 1) & 1) + 2 * (g & 1))) << 4) + 8 * (pp & 1);
  s16x8 Xf[2][4][2]; s16x4 Wl[2][3][2], Wh[2][3][2];
  const unsigned lb = ldsaddr(lds);
  auto rdfr = [&](int set, int st) {
    const unsigned b = lb + (st % 3) * 40960;
#pragma unroll
    for (int k32 = 0; k32 < 2; ++k32) {
#pragma unroll
      for (int s4 = 0; s4 < 4; ++s4) Xf[set][s4][k32] = rd128(b + aXr[k32], s4 * 2048);
#pragma unroll
      for (int m = 0; m < 3; ++m) { Wl[set][m][k32] = trd(b + aWt, m * 8192 + k32 * 4096); Wh[set][m][k32] = trd(b + aWt, m * 8192 + k32 * 4096 + 512); }
    }
  };
  f32x4 aQ[4] = {}, aK[4] = {}, aV[4] = {};
  auto mma = [&](int set, auto hook) {
#pragma unroll
    for (int k32 = 0; k32 < 2; ++k32)
#pragma unroll
      for (int s4 = 0; s4 < 4; ++s4) {
        aQ[s4] = mfma16(cat(Wl[set][0][k32], Wh[set][0][k32]), Xf[set][s4][k32], aQ[s4]);
        aK[s4] = mfma16(cat(Wl[set][1][k32], Wh[set][1][k32]), Xf[set][s4][k32], aK[s4]);
        aV[s4] = mfma16(Xf[set][s4][k32], cat(Wl[set][2][k32], Wh[set][2][k32]), aV[s4]);
        SCHED();
        hook(k32 * 4 + s4);
        SCHED();
      }
  };
  if (w == 0) {
    const rsrc_t rF = mkrs(p.Wphi + cb * 1024, 2048);
    bl16<0>(rF, lane * 16, 0, lds + 124928); bl16<0>(rF, lane * 16, 1024, lds + 124928 + 1024);
  }
  issue(0); issue(1); issue(2);
  if (tid == 256) read_epoch(p, lds);
  unsigned dummy = 0;
  {
  }
  if (w < 4) {
    WAIT_V(20);
    BAR();
    rdfr(0, 0); WAIT_L0(); SCHED();
#pragma unroll
    for (int i = 0; i < 8; ++i) {
      if (i < 6) WAIT_V(10); else if (i == 6) WAIT_V(0);
      BAR(); SCHED();
      if (i < 7) rdfr((i + 1) & 1, i + 1);
      mma(i & 1, [&](int j) { if (i < 5 && j < 3) { issue1(i + 3, 3 * j); issue1(i + 3, 3 * j + 1); issue1(i + 3, 3 * j + 2); if (j == 2) issue1(i + 3, 9); } });
      WAIT_L0(); SCHED();
    }
  } else {
    pf(APFS, APF0);
    BAR();
    rdfr(0, 0); WAIT_L0(); SCHED();
#pragma unroll
    for (int i = 0; i < 8; ++i) {
      BAR(); SCHED();
      if (i < 7) rdfr((i + 1) & 1, i + 1);
      mma(i & 1, [&](int j) { if (j < APF1) pf1(APFS + APF0 + i * APF1 + j); });
      WAIT_L0(); SCHED();
    }
  }
  asm volatile("" : "+v"(dummy));
  s16x4 wph;
#pragma unroll
  for (int j = 0; j < 4; ++j) wph[j] = *(const short*)(lds + 124928 + hh * 512 + ((4 * g + j) * 16 + r16) * 2);
  s16x4 ident;
#pragma unroll
  for (int j = 0; j < 4; ++j) ident[j] = (4 * g + j == r16) ? (short)0x3F80 : (short)0;
  const s16x4 ones = {(short)0x3F80, (short)0x3F80, (short)0x3F80, (short)0x3F80};
  f32x4 kvp = {}, kvn = {}, zp = {}, zn = {};
  const f32x4 zero = {};
  {
    f32x4 pk[4]; s16x4 pos[4], neg[4];
#pragma unroll
    for (int s4 = 0; s4 < 4; ++s4) pk[s4] = mfma16k16(wph, pk4(aK[s4][0], aK[s4][1], aK[s4][2], aK[s4][3]), zero);
    softmax_n<4>(pk, pos, neg);
    f32x4 tp[4], tn[4];
#pragma unroll
    for (int s4 = 0; s4 < 4; ++s4) { tp[s4] = mfma16k16(pos[s4], ident, zero); tn[s4] = mfma16k16(neg[s4], ident, zero); }
#pragma unroll
    for (int s2 = 0; s2 < 2; ++s2) {
      const int sa = 2 * s2, sb = 2 * s2 + 1;
      s16x8 tpb = cat(pk4(tp[sa][0], tp[sa][1], tp[sa][2], tp[sa][3]), pk4(tp[sb][0], tp[sb][1], tp[sb][2], tp[sb][3]));
      s16x8 tnb = cat(pk4(tn[sa][0], tn[sa][1], tn[sa][2], tn[sa][3]), pk4(tn[sb][0], tn[sb][1], tn[sb][2], tn[sb][3]));
      s16x8 vb = cat(pk4(aV[sa][0], aV[sa][1], aV[sa][2], aV[sa][3]), pk4(aV[sb][0], aV[sb][1], aV[sb][2], aV[sb][3]));
      kvp = mfma16(tpb, vb, kvp); kvn = mfma16(tnb, vb, kvn);
      zp = mfma16(tpb, cat(ones, ones), zp); zn = mfma16(tnb, cat(ones, ones), zn);
    }
  }
  BAR();
  float* ex = (float*)(lds + 106496);
  if (wm == 1) {
    *(f32x4*)(ex + (hh * 4 + 0) * 256 + lane * 4) = kvp; *(f32x4*)(ex + (hh * 4 + 1) * 256 + lane * 4) = kvn;
    *(f32x4*)(ex + (hh * 4 + 2) * 256 + lane * 4) = zp;  *(f32x4*)(ex + (hh * 4 + 3) * 256 + lane * 4) = zn;
  }
  const u64 tag = *(volatile LAS u64*)(lds + LDS_MISC);
  __syncthreads();
  if (wm == 0) {
    kvp += *(const f32x4*)(ex + (hh * 4 + 0) * 256 + lane * 4); kvn += *(const f32x4*)(ex + (hh * 4 + 1) * 256 + lane * 4);
    zp += *(const f32x4*)(ex + (hh * 4 + 2) * 256 + lane * 4);  zn += *(const f32x4*)(ex + (hh * 4 + 3) * 256 + lane * 4);
    const unsigned t32 = (unsigned)tag ^ (unsigned)(tag >> 32);
    const float kv8[8] = {kvp[0], kvp[1], kvp[2], kvp[3], kvn[0], kvn[1], kvn[2], kvn[3]};
    const float z8[8] = {zp[0], zp[1], zp[2], zp[3], zn[0], zn[1], zn[2], zn[3]};
#pragma unroll
    for (int j = 0; j < 4; ++j)
      st_sc1(rs, WS_KVPG + (unsigned)(((rb * 32 + head) * 4 + j) * 64 + lane) * 16, u32x4{__float_as_uint(kv8[2 * j]), t32, __float_as_uint(kv8[2 * j + 1]), t32});
    if (r16 < 4) {
      float za = z8[0], zb = z8[1];
#pragma unroll
      for (int j = 1; j < 4; ++j) if (r16 == j) { za = z8[2 * j]; zb = z8[2 * j + 1]; }
      st_sc1(rs, WS_ZPG + (unsigned)((((head * 4 + r16) * 16 + rb) * 4 + g)) * 16, u32x4{__float_as_uint(za), t32, __float_as_uint(zb), t32});
    }
  }
  {
    f32x4 pq[4]; s16x4 pos[4], neg[4];
#pragma unroll
    for (int s4 = 0; s4 < 4; ++s4) pq[s4] = mfma16k16(wph, pk4(aQ[s4][0], aQ[s4][1], aQ[s4][2], aQ[s4][3]), zero);
    softmax_n<4>(pq, pos, neg);
    if (w == 3) kvreduce(p, rs, cb * 4 + (rb >> 2), rb & 3, tag);
    a3_prefetch(p, cb, lds);
    const unsigned t32 = (unsigned)tag ^ (unsigned)(tag >> 32);
    u32x2 kq[4], zq[4];
    for (unsigned spins = 0;; ++spins) {
#pragma unroll
      for (int j = 0; j < 4; ++j) {
        zq[j] = ld_sc1_64(rs, WS_ZFG + (unsigned)((head * 4 + j) * 4 + g) * 8);
        kq[j] = ld_sc1_64(rs, WS_KVFG + (unsigned)((head * 4 + j) * 64 + lane) * 8);
      }
      bool ok = true;
#pragma unroll
      for (int j = 0; j < 4; ++j) ok = ok && zq[j][1] == t32 && kq[j][1] == t32;
      if (__all(ok)) break;
      __builtin_amdgcn_s_sleep(POLL_SLEEP);
      if (spins > SPIN_CAP) { if (lane == 0) st_flag(p.ws, SY_TMO, tag); break; }
    }
    const s16x8 kf = __builtin_bit_cast(s16x8, u32x4{kq[0][0], kq[1][0], kq[2][0], kq[3][0]});
    const s16x8 zz = __builtin_bit_cast(s16x8, u32x4{zq[0][0], zq[1][0], zq[2][0], zq[3][0]});
    f32x4 num[4], den[4];
#pragma unroll
    for (int s4 = 0; s4 < 4; ++s4) { const s16x8 phv = cat(pos[s4], neg[s4]); num[s4] = mfma16(kf, phv, zero); den[s4] = mfma16(zz, phv, zero); }
#pragma unroll
    for (int s4 = 0; s4 < 4; ++s4) {
      const float r = __builtin_amdgcn_rcpf(den[s4][0] + 1e-6f);
      const int stile = rb * 8 + wm * 4 + s4;
      st_sc1_64(rs, WS_PHIQ + (unsigned)((stile * 32 + head) * 64 + lane) * 8,
                __builtin_bit_cast(u32x2, pk4(num[s4][0] * r, num[s4][1] * r, num[s4][2] * r, num[s4][3] * r)));
    }
  }
}

constexpr int A3_WO = 0, A3_KV = 65536;
DI void a3_prefetch(const Params& p, int nb, char* lds) {
  const int lane = threadIdx.x & 63, w = __builtin_amdgcn_readfirstlane(threadIdx.x >> 6);
#pragma unroll
  for (int i = 0; i < 8; ++i) {
    const int k = 8 * i + (lane >> 3);
    glds16(p.Wo + (size_t)(w * 64 + k) * 512 + nb * 64 + (((lane & 7) ^ (4 * ((k >> 1) & 1) + 2 * ((k >> 2) & 1))) << 3), lds + A3_WO + (w * 8 + i) * 1024);
  }
}
DI void a3(const Params& p, rsrc_t rs, int rb, int nb, u64 tag, char* lds) {
  const int tid = threadIdx.x, lane = tid & 63, w = __builtin_amdgcn_readfirstlane(tid >> 6);
  const int g = lane >> 4, r16 = lane & 15, q = r16 >> 2, pp = lane & 3;
  {
    volatile LAS unsigned* rdy = (volatile LAS unsigned*)(lds + LDS_MISC + 12);
    if (w == 0) {
      wait_flags(p.ws, SY_A1 + rb * 64, 8, tag, lane);
      if (lane == 0) *rdy = 1u;
    }
    else { while (*rdy == 0u) __builtin_amdgcn_s_sleep(1); }
    asm volatile("" ::: "memory");
  }
  const int sp = w >> 1, kh = w & 1;
  u32x2 oq[2][16];
#pragma unroll
  for (int h = 0; h < 16; ++h)
#pragma unroll
    for (int t = 0; t < 2; ++t)
      oq[t][h] = ld_sc1_64(rs, WS_PHIQ + (unsigned)(((rb * 8 + 2 * sp + t) * 32 + ((16 * kh + h + 4 * nb) & 31)) * 64 + lane) * 8);
  f32x4 ac2[2][4] = {};
  const int krow = 4 * g + q;
  const int swz = 4 * ((krow >> 1) & 1) + 2 * ((krow >> 2) & 1);
  const unsigned lb = ldsaddr(lds);
  unsigned aWo[4];
#pragma unroll
  for (int nt = 0; nt < 4; ++nt) aWo[nt] = lb + A3_WO + krow * 128 + (((2 * nt + (pp >> 1)) ^ swz) << 4) + 8 * (pp & 1);
  s16x4 wl[4][2][4], wh[4][2][4];
#pragma unroll
  for (int b4 = 0; b4 < 4; ++b4) {
    const int bb = (4 * kh + b4 + nb) & 7;
#pragma unroll
    for (int hp = 0; hp < 2; ++hp)
#pragma unroll
      for (int nt = 0; nt < 4; ++nt) { wl[b4][hp][nt] = trd(aWo[nt] + bb * 8192, hp * 4096); wh[b4][hp][nt] = trd(aWo[nt] + bb * 8192, hp * 4096 + 2048); }
  }
  WAIT_L0(); SCHED();
#pragma unroll
  for (int b4 = 0; b4 < 4; ++b4) {
#pragma unroll
    for (int hp = 0; hp < 2; ++hp)
#pragma unroll
      for (int t = 0; t < 2; ++t) {
        const s16x8 of = __builtin_bit_cast(s16x8, u32x4{oq[t][4 * b4 + 2 * hp][0], oq[t][4 * b4 + 2 * hp][1], oq[t][4 * b4 + 2 * hp + 1][0], oq[t][4 * b4 + 2 * hp + 1][1]});
#pragma unroll
        for (int nt = 0; nt < 4; ++nt) ac2[t][nt] = mfma16(cat(wl[b4][hp][nt], wh[b4][hp][nt]), of, ac2[t][nt]);
      }
    SCHED();
  }
  f32x4 acc[4];
  {
    float* exo = (float*)(lds + 106496) + w * 1024 + lane * 4;
    const float* exi = (const float*)(lds + 106496) + (w ^ 1) * 1024 + lane * 4;
#pragma unroll
    for (int nt = 0; nt < 4; ++nt) { *(f32x4*)(exo + nt * 256) = kh ? ac2[0][nt] : ac2[1][nt]; acc[nt] = kh ? ac2[1][nt] : ac2[0][nt]; }
    WAIT_L0(); BAR(); asm volatile("" ::: "memory");
#pragma unroll
    for (int nt = 0; nt < 4; ++nt) acc[nt] += *(const f32x4*)(exi + nt * 256);
  }
  const unsigned doff = (unsigned)((rb * 128 + w * 16 + r16) * 1024 + nb * 64 + (g & 1) * 16 + (g >> 1) * 8) * 2u;
#pragma unroll
  for (int np = 0; np < 2; ++np) {
    const unsigned a0 = pk2(acc[2 * np][0], acc[2 * np][1]), a1 = pk2(acc[2 * np][2], acc[2 * np][3]);
    const unsigned b0 = pk2(acc[2 * np + 1][0], acc[2 * np + 1][1]), b1 = pk2(acc[2 * np + 1][2], acc[2 * np + 1][3]);
    auto x0 = __builtin_amdgcn_permlane16_swap(a0, b0, false, false);
    auto x1 = __builtin_amdgcn_permlane16_swap(a1, b1, false, false);
    __builtin_amdgcn_raw_buffer_store_b128(u32x4{x0[0], x1[0], x0[1], x1[1]}, mkrs(p.out, 4194304u), doff + np * 64u, 0, OUTA_AUX);
  }
}

__global__ __launch_bounds__(512, 2) void fused(Params p) {
  extern __shared__ __attribute__((aligned(16))) char lds[];
  const int tid = threadIdx.x, lane = tid & 63, w = __builtin_amdgcn_readfirstlane(tid >> 6);
  const int b = blockIdx.x, xcd = b & 7, i = b >> 3;
  const rsrc_t rs = __builtin_amdgcn_make_buffer_rsrc((void*)p.ws, 0, WS_BYTES, 0x00020000);
  volatile LAS u64* tagp = (volatile LAS u64*)(lds + LDS_MISC);
  u64 tag;
  const int tb64 = 4 * xcd + (i >> 3), eb = i & 7;
  if (i < 16) {
    const int tb = 2 * xcd + (i >> 3), ri = (i >> 2) & 1, cb = i & 3;
    bbig(p, rs, tb, ri, cb, lds);
    __syncthreads();
    tag = *tagp;
    if (tid == 0) st_flag(p.ws, SY_PQ + tb * 64 + (ri * 4 + cb) * 8, tag);
    bsmall_prefetch(p, eb, lds);
  } else {
    const int a = i - 16, rb = 2 * xcd + (a >> 3), cb = a & 7;
    a1(p, rs, rb, cb, lds);
    tag = *tagp;
    WAIT_V(0);
    if (lane == 0) {
      const unsigned old = __hip_atomic_fetch_add((LAS unsigned*)(lds + LDS_MISC + 8), 1u, __ATOMIC_RELAXED, __HIP_MEMORY_SCOPE_WORKGROUP);
      if (old == 7u) st_flag(p.ws, SY_A1 + rb * 64 + cb * 8, tag);
    }
    BAR();
    a3(p, rs, rb, cb, tag, lds);
  }
  if (i < 16) {
    bsmall(p, rs, tb64, eb, tag, lds);
  }
  if (b == 0 && w == 0) {
    for (int c = 0; c < 4; ++c) wait_flags(p.ws, SY_START + c * 512, 64, tag, lane);
    if (lane == 0) st_flag(p.ws, SY_EPOCH, tag);
  }
}

extern "C" void kernel_launch(void* const* d_in, const int* in_sizes, int n_in, void* d_out, int out_size, void* d_ws, size_t ws_size, hipStream_t stream) {
  Params p;
  p.Ax = (const u16*)d_in[0]; p.Wqkv = (const u16*)d_in[1]; p.Wphi = (const u16*)d_in[2]; p.Wo = (const u16*)d_in[3];
  p.Bx = (const u16*)d_in[4]; p.Wsr = (const u16*)d_in[5]; p.Wsi = (const u16*)d_in[6]; p.Wdr = (const u16*)d_in[7]; p.Wdi = (const u16*)d_in[8];
  p.out = (u16*)d_out; p.ws = (char*)d_ws;
  hipFuncSetAttribute((const void*)fused, hipFuncAttributeMaxDynamicSharedMemorySize, LDS_BYTES);
  fused<<<256, 512, LDS_BYTES, stream>>>(p);
}
```
